```python
import torch

def _softmax(x, axis=-1):
    return torch.softmax(x, dim=axis)
def _gelu(x):
    return torch.nn.functional.gelu(x, approximate="tanh")
def _silu(x):
    return torch.nn.functional.silu(x)
def _sigmoid(x):
    return torch.sigmoid(x)
def _layernorm(x, axis=-1, eps=1e-5):
    m = x.mean(dim=axis, keepdim=True)
    v = x.var(dim=axis, keepdim=True, unbiased=False)
    return (x - m) / torch.sqrt(v + eps)
def _scatter_add(tgt, idx, src, axis=0):
    return tgt.clone().index_add_(axis, idx, src)
def _segment_sum(src, seg, n):
    out = torch.zeros((n,) + src.shape[1:], dtype=src.dtype)
    return out.index_add_(0, seg, src)
def _pad(x, pw):
    flat = tuple(v for pair in reversed(pw) for v in pair)
    return torch.nn.functional.pad(x, flat)

kernel_name = 'gated_mix__sliding_block_cached__glu_gated_geglu__decode'

def setup_inputs():

    _w = (lambda a: (lambda t: t.to(torch.bfloat16) if t.is_floating_point() else t)(torch.as_tensor(a)))
    def _A_setup():
        B, Sq, D = 1, 1, 640
        Hq, Hkv, Sk, Dh = 32, 32, 4096, 192
        assert Hq % Hkv == 0, (Hq, Hkv)
        return {
            "x": _w((lambda *s: torch.randn(*s, dtype=torch.bfloat16) * 0.05)(B, Sq, D)),
            "k_cache": _w((lambda *s: torch.randn(*s, dtype=torch.bfloat16) * 6.4)(B, Hkv, Sk, Dh)),
            "v_cache": _w((lambda *s: torch.randn(*s, dtype=torch.bfloat16) * 0.05)(B, Hkv, Sk, Dh)),
            "wq": _w((lambda *s: torch.randn(*s, dtype=torch.bfloat16) * 6.4)(D, Hq * Dh) * (D ** -0.5)),
            "wo": _w((lambda *s: torch.randn(*s, dtype=torch.bfloat16) * 0.05)(Hq * Dh, D) * ((Hq * Dh) ** -0.5)),
        }
    _A_in = _A_setup()

    def _B_setup():
        B, S, Dm, Df = 1, 1, 640, 1536
        return {
            "x": _w((lambda *s: torch.randn(*s, dtype=torch.bfloat16) * 0.05)(B, S, Dm)),
            "w_gate": _w((lambda *s: torch.randn(*s, dtype=torch.bfloat16) * 0.8)(Dm, Df) * (Dm ** -0.5)),
            "w_up": _w((lambda *s: torch.randn(*s, dtype=torch.bfloat16) * 0.8)(Dm, Df) * (Dm ** -0.5)),
            "w_down": _w((lambda *s: torch.randn(*s, dtype=torch.bfloat16) * 0.8)(Df, Dm) * (Df ** -0.5)),
        }
    _B_in = _B_setup()

    return {
        "A_x": _A_in["x"],
        "A_k_cache": _A_in["k_cache"],
        "A_v_cache": _A_in["v_cache"],
        "A_wq": _A_in["wq"],
        "A_wo": _A_in["wo"],
        "B_x": _B_in["x"],
        "B_w_gate": _B_in["w_gate"],
        "B_w_up": _B_in["w_up"],
        "B_w_down": _B_in["w_down"],
        "gate_w": _w((lambda *s: torch.randn(*s, dtype=torch.bfloat16) * 0.05)(640) * 16.0),
    }

def reference(A_x, A_k_cache, A_v_cache, A_wq, A_wo, B_x, B_w_gate, B_w_up, B_w_down, gate_w):
    def _A_ref(x, k_cache, v_cache, wq, wo):
        B, Sq, D = x.shape
        Hq, Dh = 32, 192
        Hkv, Sk = k_cache.shape[1], k_cache.shape[2]
        W = min(4096, max(256, Sk // 4))
        rep = Hq // Hkv
        q = (lambda x, *d: x.permute(*d))((x @ wq).reshape(B, Sq, Hq, Dh), 0, 2, 1, 3)
        k = (lambda x, n, axis: torch.repeat_interleave(x, n, dim=axis))(k_cache[:, :, Sk - W:, :], rep, axis=1)
        v = (lambda x, n, axis: torch.repeat_interleave(x, n, dim=axis))(v_cache[:, :, Sk - W:, :], rep, axis=1)
        scores = torch.einsum("bhqd,bhkd->bhqk", q, k) * (Dh ** -0.5)
        p = _softmax(scores, axis=-1)
        o = torch.einsum("bhqk,bhkd->bhqd", p, v)
        return (lambda x, *d: x.permute(*d))(o, 0, 2, 1, 3).reshape(B, Sq, Hq * Dh) @ wo

    def _B_ref(x, w_gate, w_up, w_down):
        g = torch.einsum("bsd,df->bsf", x, w_gate)
        u = torch.einsum("bsd,df->bsf", x, w_up)
        h = _gelu(g) * u
        return torch.einsum("bsf,fd->bsd", h, w_down)

    x = A_x
    g = _sigmoid((x * gate_w).sum(-1, keepdims=True))
    a = _A_ref(x, A_k_cache, A_v_cache, A_wq, A_wo)
    b = _B_ref(x, B_w_gate, B_w_up, B_w_down)
    return g * a + (1 - g) * b
```

```cpp
#include <hip/hip_runtime.h>
#include <hip/hip_bf16.h>
typedef unsigned short u16;
__device__ __forceinline__ float bf2f(u16 v){ return __uint_as_float(((unsigned)v)<<16); }
__device__ __forceinline__ u16 f2bf(float f){ unsigned u=__float_as_uint(f); return (u16)((u+0x7fffu+((u>>16)&1u))>>16); }
__device__ __forceinline__ float rbf(float f){ return bf2f(f2bf(f)); }
constexpr int D=640,H=32,SK=4096,W=1024,DH=192,DF=1536;
__global__ void kA(const u16* x,const u16* wq,const u16* wg,const u16* wu,float* q,float* hid){
  int c=blockIdx.x*64+threadIdx.x;
  if(c<H*DH){ float a=0; for(int k=0;k<D;k++) a+=bf2f(x[k])*bf2f(wq[(size_t)k*H*DH+c]); q[c]=rbf(a);}
  else { int j=c-H*DH; float g=0,u=0; for(int k=0;k<D;k++){ float xv=bf2f(x[k]); g+=xv*bf2f(wg[(size_t)k*DF+j]); u+=xv*bf2f(wu[(size_t)k*DF+j]); }
    g=rbf(g); u=rbf(u);
    float t=0.7978845608028654f*(g+0.044715f*g*g*g);
    float ge=rbf(0.5f*g*(1.f+tanhf(t)));
    hid[j]=rbf(ge*u);
  }
}
__global__ void kB(const float* q,const u16* kc,const u16* vc,float* o){
  __shared__ float s[W]; __shared__ float red[256]; __shared__ float qs[DH];
  int h=blockIdx.x,t=threadIdx.x;
  if(t<DH) qs[t]=q[h*DH+t];
  __syncthreads();
  const u16* kb=kc+((size_t)h*SK+(SK-W))*DH; const u16* vb=vc+((size_t)h*SK+(SK-W))*DH;
  float mx=-INFINITY;
  for(int j=t;j<W;j+=256){ float a=0; for(int d=0;d<DH;d++) a+=qs[d]*bf2f(kb[(size_t)j*DH+d]); a=rbf(rbf(a)*0.07216878364870322f); s[j]=a; mx=fmaxf(mx,a);}
  red[t]=mx; __syncthreads();
  for(int k=128;k>0;k>>=1){ if(t<k) red[t]=fmaxf(red[t],red[t+k]); __syncthreads(); }
  mx=red[0]; __syncthreads();
  float sm=0; for(int j=t;j<W;j+=256){ float e=__expf(s[j]-mx); s[j]=e; sm+=e; }
  red[t]=sm; __syncthreads();
  for(int k=128;k>0;k>>=1){ if(t<k) red[t]+=red[t+k]; __syncthreads(); }
  sm=red[0];
  if(t<DH){ float a=0; for(int j=0;j<W;j++) a+=rbf(s[j]/sm)*bf2f(vb[(size_t)j*DH+t]); o[h*DH+t]=rbf(a);}
}
__global__ void kC(const u16* x,const u16* gw,const float* o,const float* hid,const u16* wo,const u16* wd,u16* out){
  __shared__ float ra[256],rb[256];
  int c=blockIdx.x*64+(threadIdx.x&63), p=threadIdx.x>>6;
  float a=0,b=0;
  for(int k=p;k<H*DH;k+=4) a+=o[k]*bf2f(wo[(size_t)k*D+c]);
  for(int k=p;k<DF;k+=4) b+=hid[k]*bf2f(wd[(size_t)k*D+c]);
  ra[threadIdx.x]=a; rb[threadIdx.x]=b; __syncthreads();
  if(p==0){ int t=threadIdx.x; a=rbf(ra[t]+ra[t+64]+ra[t+128]+ra[t+192]); b=rbf(rb[t]+rb[t+64]+rb[t+128]+rb[t+192]);
    float z=0; for(int k=0;k<D;k++) z+=rbf(bf2f(x[k])*bf2f(gw[k])); z=rbf(z);
    float g=rbf(1.f/(1.f+__expf(-z)));
    out[c]=f2bf(rbf(g*a)+rbf(rbf(1.f-g)*b)); }
}
extern "C" void kernel_launch(void* const* d_in, const int* in_sizes, int n_in, void* d_out, int out_size, void* d_ws, size_t ws_size, hipStream_t stream) {
  const u16* x=(const u16*)d_in[0]; const u16* kc=(const u16*)d_in[1]; const u16* vc=(const u16*)d_in[2];
  const u16* wq=(const u16*)d_in[3]; const u16* wo=(const u16*)d_in[4];
  const u16* wg=(const u16*)d_in[6]; const u16* wu=(const u16*)d_in[7]; const u16* wd=(const u16*)d_in[8]; const u16* gw=(const u16*)d_in[9];
  float* q=(float*)d_ws; float* hid=q+H*DH; float* o=hid+DF;
  kA<<<(H*DH+DF)/64,64,0,stream>>>(x,wq,wg,wu,q,hid);
  kB<<<H,256,0,stream>>>(q,kc,vc,o);
  kC<<<D/64,256,0,stream>>>(x,gw,o,hid,wo,wd,(u16*)d_out);
}
```

```cpp
#include <hip/hip_runtime.h>
#include <hip/hip_bf16.h>

typedef unsigned short u16;
typedef unsigned u32;
typedef unsigned long long u64;
typedef u32 u32x4 __attribute__((ext_vector_type(4)));
typedef u32 u32x2 __attribute__((ext_vector_type(2)));
typedef float f32x4 __attribute__((ext_vector_type(4)));
typedef short s16x4 __attribute__((ext_vector_type(4)));
typedef __bf16 bf16x8 __attribute__((ext_vector_type(8)));
#define GAS __attribute__((address_space(1)))
#define LAS __attribute__((address_space(3)))

#define NT_W 0
#define NT_A 0
#define NT_O 0
#define PACE16 48
#define CODE_PF 1
#define WINDOW 45
#define BURST 6

constexpr int D = 640, H = 32, SK = 4096, WN = 1024, DH = 192, DF = 1536;
constexpr int L_XQ = 0;
constexpr int L_K  = 30720;
constexpr int L_V  = 79872;
constexpr int L_G  = 129024;
constexpr int L_D  = 144384;
constexpr int L_S  = 152576;
constexpr int L_XS = L_S, L_XG = L_S + 384, L_QS = L_S + 640, L_PS = L_S + 1024, L_OS = L_S + 1280, L_HS = L_S + 1536, L_MX = L_S + 1600;
constexpr int L_PROG = L_S + 1792, L_FL = L_PROG + 256, LDS_BYTES = L_FL + 256;
enum { F_Q = 0, F_MX, F_P, F_O, F_H, F_T1, F_TAG, F_THI, F_TLO, F_GATE, F_GATE1 };
constexpr size_t W_Q = 4096, W_G = W_Q + 32 * 4 * 192 * 8, W_O = W_G + 16 * 16 * 96 * 8, W_ML = W_O + 32 * 8 * 192 * 8, W_P = W_ML + 32 * 16 * 8;
constexpr int N_A = 30, N_K = N_A + 48, N_V = N_K + 48, N_G = N_V + 15, N_D = N_G + 8, N_O = N_D + 30;
constexpr int SPIN_LDS = 1 << 18, SPIN_MEM = 1 << 15;

#define STAMP(i) do {} while (0)
#define TAKE(v) do {} while (0)
#define GIVE(i, v) do {} while (0)

__device__ __forceinline__ float bf2f(u32 v) { return __uint_as_float(v << 16); }
__device__ __forceinline__ u32 f2bf(float f) { u32 u = __float_as_uint(f); return (u + 0x7fffu + ((u >> 16) & 1u)) >> 16; }
__device__ __forceinline__ float rbf(float f) { return __uint_as_float(f2bf(f) << 16); }
__device__ __forceinline__ u32 rfl(u32 v) { return __builtin_amdgcn_readfirstlane(v); }
template <int CTRL> __device__ __forceinline__ float dpp(float x) {
    return __builtin_bit_cast(float, __builtin_amdgcn_mov_dpp(__builtin_bit_cast(int, x), CTRL, 0xf, 0xf, true));
}
__device__ __forceinline__ float xrow_max(float x) {
    auto s = __builtin_amdgcn_permlane16_swap(__float_as_uint(x), __float_as_uint(x), false, false);
    x = fmaxf(__uint_as_float(s[0]), __uint_as_float(s[1]));
    auto t = __builtin_amdgcn_permlane32_swap(__float_as_uint(x), __float_as_uint(x), false, false);
    return fmaxf(__uint_as_float(t[0]), __uint_as_float(t[1]));
}
__device__ __forceinline__ float xrow_sum(float x) {
    auto s = __builtin_amdgcn_permlane16_swap(__float_as_uint(x), __float_as_uint(x), false, false);
    x = __uint_as_float(s[0]) + __uint_as_float(s[1]);
    auto t = __builtin_amdgcn_permlane32_swap(__float_as_uint(x), __float_as_uint(x), false, false);
    return __uint_as_float(t[0]) + __uint_as_float(t[1]);
}
constexpr int ROR8 = 0x128, ROR4 = 0x124, ROR2 = 0x122, ROR1 = 0x121;
__device__ __forceinline__ f32x4 mfma32(u32x4 a, u32x4 b, f32x4 c) {
    return __builtin_amdgcn_mfma_f32_16x16x32_bf16(__builtin_bit_cast(bf16x8, a), __builtin_bit_cast(bf16x8, b), c, 0, 0, 0);
}
__device__ __forceinline__ f32x4 mfma16(u32x2 a, u32x2 b, f32x4 c) {
    return __builtin_amdgcn_mfma_f32_16x16x16bf16_1k(__builtin_bit_cast(s16x4, a), __builtin_bit_cast(s16x4, b), c, 0, 0, 0);
}
__device__ __forceinline__ u32x2 trd(LAS unsigned char* p) {
    return __builtin_bit_cast(u32x2, __builtin_amdgcn_ds_read_tr16_b64_v4i16((LAS s16x4*)p));
}
__device__ __forceinline__ u32x4 trd2(LAS unsigned char* p, int off2) {
    u32x2 a = trd(p), b = trd(p + off2);
    return u32x4{a.x, a.y, b.x, b.y};
}
template <int SLEEP> __device__ __forceinline__ void wait_ge(u32 addr, u32 need, u32& seen) {
    u32 tmp, cnt = SPIN_LDS;
    if (SLEEP)
        asm volatile("s_cmp_ge_u32 %0, %4\n\ts_cbranch_scc1 2f\n"
                     "1:\n\tds_read_b32 %1, %3\n\ts_waitcnt lgkmcnt(0)\n\tv_readfirstlane_b32 %0, %1\n\ts_cmp_ge_u32 %0, %4\n\ts_cbranch_scc1 2f\n"
                     "\ts_sleep %5\n\ts_sub_u32 %2, %2, 1\n\ts_cbranch_scc0 1b\n2:"
                     : "+s"(seen), "=&v"(tmp), "+s"(cnt) : "v"(addr), "s"(need), "i"(SLEEP) : "memory", "scc");
    else
        asm volatile("s_cmp_ge_u32 %0, %4\n\ts_cbranch_scc1 2f\n"
                     "1:\n\tds_read_b32 %1, %3\n\ts_waitcnt lgkmcnt(0)\n\tv_readfirstlane_b32 %0, %1\n\ts_cmp_ge_u32 %0, %4\n\ts_cbranch_scc1 2f\n"
                     "\ts_sub_u32 %2, %2, 1\n\ts_cbranch_scc0 1b\n2:"
                     : "+s"(seen), "=&v"(tmp), "+s"(cnt) : "v"(addr), "s"(need) : "memory", "scc");
}
template <int SLEEP> __device__ __forceinline__ void wait_flag(u32 addr, u32 need) { u32 seen = 0; wait_ge<SLEEP>(addr, need, seen); }
__device__ __forceinline__ void lds_inc(LAS u32* p, int lane) {
    asm volatile("" ::: "memory");
    if (lane == 0) __hip_atomic_fetch_add(p, 1u, __ATOMIC_RELAXED, __HIP_MEMORY_SCOPE_WORKGROUP);
    asm volatile("" ::: "memory");
}
__device__ __forceinline__ void put(GAS u64* g, float v, u32 thi, u32 tlo) {
    u32 lo = __builtin_amdgcn_alignbit(tlo, __float_as_uint(v) + 0x80u, 8);
    __hip_atomic_store(g, ((u64)thi << 32) | lo, __ATOMIC_RELAXED, __HIP_MEMORY_SCOPE_AGENT);
}
__device__ __forceinline__ u64 get(GAS u64* g) { return __hip_atomic_load(g, __ATOMIC_RELAXED, __HIP_MEMORY_SCOPE_AGENT); }
__device__ __forceinline__ u32 bad(u64 x, u32 thi, u32 tlo) { return ((u32)(x >> 32) ^ thi) | (((u32)x >> 24) ^ tlo); }
__device__ __forceinline__ float val(u64 x) { return __uint_as_float((u32)x << 8); }
#define SPARG(i)
template <int N> __device__ __forceinline__ void sweep(GAS u64* p, int S, u64 (&v)[N], u32 thi, u32 tlo
) {
    int it = 0;
#pragma unroll 1
    for (; it < SPIN_MEM; ++it) {
        u32 bd = 0;
#pragma unroll
        for (int i = 0; i < N; ++i) v[i] = get(p + S * i);
#pragma unroll
        for (int i = 0; i < N; ++i) bd |= bad(v[i], thi, tlo);
        if (__all(bd == 0)) break;
    }
}
#define S_W ""
#define S_A ""
#define S_O ""
#define GL3(POL, v0, v1, v2, base, ldsaddr) \
    asm volatile("s_mov_b32 m0, %4\n\ts_nop 0\n\tglobal_load_lds_dwordx4 %0, %3" POL "\n\tglobal_load_lds_dwordx4 %1, %3 offset:1024" POL "\n\tglobal_load_lds_dwordx4 %2, %3 offset:2048" POL \
                 :: "v"(v0), "v"(v1), "v"(v2), "s"(base), "s"(ldsaddr) : "memory")
#define GL2(POL, v0, v1, base, ldsaddr) \
    asm volatile("s_mov_b32 m0, %3\n\ts_nop 0\n\tglobal_load_lds_dwordx4 %0, %2" POL "\n\tglobal_load_lds_dwordx4 %1, %2 offset:1024" POL \
                 :: "v"(v0), "v"(v1), "s"(base), "s"(ldsaddr) : "memory")

__device__ __forceinline__ void xw_phase(LAS unsigned char* lds, int xoff, int woff, int nu, u32 p0, u32 aprog, int g, int trow, int p4, float& r0, float& r1) {
    u32 seen = 0;
    f32x4 acc[6];
#pragma unroll
    for (int c = 0; c < 6; ++c) acc[c] = f32x4{0, 0, 0, 0};
    LAS unsigned char* xp = lds + xoff + 8 * g;
    LAS unsigned char* bp = lds + woff + trow * 192 + 8 * p4;
#pragma unroll 1
    for (int u = 0; u < nu; ++u) {
        const u32x2 a = *(LAS u32x2*)xp;
        wait_ge<1>(aprog, p0 + 3 * (u + 1), seen);
#pragma unroll
        for (int c = 0; c < 6; ++c) acc[c] = mfma16(a, trd(bp + 32 * c), acc[c]);
        xp += 32; bp += 3072;
    }
    r0 = g == 0 ? acc[0][0] : (g == 1 ? acc[1][0] : (g == 2 ? acc[2][0] : acc[3][0]));
    r1 = g == 0 ? acc[4][0] : acc[5][0];
}

__global__ void __launch_bounds__(512, 2)
fused_decode(const u16* __restrict__ x, const u16* __restrict__ kc, const u16* __restrict__ vc, const u16* __restrict__ wq, const u16* __restrict__ wo,
             const u16* __restrict__ wg, const u16* __restrict__ wu, const u16* __restrict__ wd, const u16* __restrict__ gw,
             u16* __restrict__ out, unsigned char* __restrict__ ws
             ) {
    extern __shared__ __attribute__((aligned(16))) unsigned char lds_[];
    LAS unsigned char* lds = (LAS unsigned char*)lds_;
    const int tid = threadIdx.x, lane = tid & 63;
    const int wave = rfl(tid >> 6);
    const int b = blockIdx.x;
    const int h = (b & 7) + 8 * (b >> 6), s = (b >> 3) & 7;
    const int G = (h & 7) + 8 * (h >> 4), gi = 8 * ((h >> 3) & 1) + s;
    const int rh = s & 1, cq = s >> 1;
    volatile LAS u32* prog = (volatile LAS u32*)(lds + L_PROG);
    volatile LAS u32* fl = (volatile LAS u32*)(lds + L_FL);
    const u32 aprog = (u32)(size_t)lds + L_PROG, afl = (u32)(size_t)lds + L_FL;
    asm volatile("fd_code_begin:");
    TAKE(ts0);
    if (wave == 6) { prog[lane] = 0; fl[lane] = 0; asm volatile("s_waitcnt lgkmcnt(0)" ::: "memory"); }
    __builtin_amdgcn_s_barrier();
    asm volatile("" ::: "memory");

    const int g = lane >> 4, c16 = lane & 15, q4 = c16 >> 2, p4 = lane & 3;

    if (__builtin_expect(wave == 7, 1)) {
        __builtin_amdgcn_s_setprio(3);
        const u32 t0 = (u32)__builtin_amdgcn_s_memrealtime();
        u32 issued = 0;
        const u32 lbase = (u32)(size_t)lds;
        auto publish = [&]() {
            u32 r = __builtin_amdgcn_s_getreg(7 | (31 << 11));
            u32 vm = (r & 15u) | ((r >> 18) & 0x30u);
            prog[lane] = issued - vm;
            return vm;
        };
        auto pace = [&]() {
            const int target = ((int)issued - BURST) * PACE16;
#pragma unroll 1
            for (;;) {
                const u32 vm = publish();
                const int now = (int)(((u32)__builtin_amdgcn_s_memrealtime() - t0) << 4);
                if (now >= target && vm <= WINDOW) break;
            }
        };
        u32 r12[3], c12[3];
#pragma unroll
        for (int i = 0; i < 3; ++i) { const u32 p = 64 * i + lane; r12[i] = p / 12; c12[i] = (p % 12) * 16 - 1024 * i; }
        TAKE(ts1);
        {
            const char* base = (const char*)wq + (size_t)(160 * (s & 3)) * 12288 + 384 * h + 192 * (s >> 2);
            const u32 v0 = r12[0] * 12288 + c12[0], v1 = r12[1] * 12288 + c12[1], v2 = r12[2] * 12288 + c12[2];
            u32 la = lbase + L_XQ;
#pragma unroll 1
            for (int u = 0; u < 10; ++u) { pace(); GL3(S_A, v0, v1, v2, base, la); issued += 3; base += 16 * 12288; la += 3072; }
        }
        TAKE(tsA);
        {
            const char* base = (const char*)kc + ((size_t)h * SK + (SK - WN) + 128 * s) * 384;
            u32 kv[6];
#pragma unroll
            for (int i = 0; i < 6; ++i) { const u32 p = 64 * i + lane, key = p / 24, u = p % 24; kv[i] = key * 384 + ((u ^ ((key >> 1) & 7)) * 16) - 1024 * (i % 3); }
            u32 la = lbase + L_K;
#pragma unroll 1
            for (int u = 0; u < 8; ++u) {
                pace(); GL3(S_W, kv[0], kv[1], kv[2], base, la); issued += 3;
                pace(); GL3(S_W, kv[3], kv[4], kv[5], base, la + 3072); issued += 3;
                base += 6144; la += 6144;
            }
        }
        TAKE(tsK);
        {
            const char* base = (const char*)vc + ((size_t)h * SK + (SK - WN) + 128 * s) * 384;
            u32 vv[3];
#pragma unroll
            for (int i = 0; i < 3; ++i) { const u32 p = 64 * i + lane, row = p / 24, u = p % 24; vv[i] = row * 384 + ((u ^ (((row >> 1) & 3) << 1)) * 16) - 1024 * i; }
            u32 la = lbase + L_V;
#pragma unroll 1
            for (int u = 0; u < 16; ++u) { pace(); GL3(S_W, vv[0], vv[1], vv[2], base, la); issued += 3; base += 3072; la += 3072; }
        }
        TAKE(tsV);
        {
            const char* base = (const char*)((gi & 1) ? wu : wg) + (size_t)(80 * (gi >> 1)) * 3072 + 192 * G;
            const u32 v0 = r12[0] * 3072 + c12[0], v1 = r12[1] * 3072 + c12[1], v2 = r12[2] * 3072 + c12[2];
            u32 la = lbase + L_G;
#pragma unroll 1
            for (int u = 0; u < 5; ++u) { pace(); GL3(S_A, v0, v1, v2, base, la); issued += 3; base += 16 * 3072; la += 3072; }
        }
        {
            u32 ov[5];
#pragma unroll
            for (int i = 0; i < 5; ++i) { const u32 p = 64 * i + lane; ov[i] = (p / 20) * 1280 + (p % 20) * 16 - 1024 * (i % 3); }
            const char* base = (const char*)wd + (size_t)(96 * G + 24 * (2 * (gi >> 3) + rh)) * 1280 + 320 * cq;
            u32 la = lbase + L_D;
            pace(); GL3(S_O, ov[0], ov[1], ov[2], base, la); issued += 3;
            pace(); GL2(S_O, ov[3], ov[4], base, la + 3072); issued += 2;
            {
                const u32 p = 128 + (lane & 31);
                const u32 o2 = (p / 20) * 1280 + (p % 20) * 16 - 2048;
                pace(); GL3(S_O, ov[0], ov[1], o2, base + 16 * 1280, la + 5120); issued += 3;
            }
            wait_flag<0>(afl + 4 * F_T1, 1);
            base = (const char*)wo + (size_t)(192 * h + 96 * rh) * 1280 + 320 * cq;
            la = lbase + L_XQ;
#pragma unroll 1
            for (int u = 0; u < 6; ++u) {
                pace(); GL3(S_O, ov[0], ov[1], ov[2], base, la); issued += 3;
                pace(); GL2(S_O, ov[3], ov[4], base, la + 3072); issued += 2;
                base += 16 * 1280; la += 5120;
            }
        }
        TAKE(ts9);
#pragma unroll 1
        for (int it = 0; it < SPIN_LDS; ++it) if (publish() == 0) break;
        asm volatile("s_waitcnt vmcnt(0)" ::: "memory");
        prog[lane] = N_O;
        TAKE(ts10); GIVE(0, ts0); GIVE(1, ts1); GIVE(9, ts9); GIVE(10, ts10); GIVE(13, tsA); GIVE(14, tsK); GIVE(15, tsV);
        return;
    }

    if (wave == 6) {
        u32x2 code_pf = {0, 0};
        {
            u64 pc; u32 beg, end;
            asm volatile("s_getpc_b64 %0\n1:\n\ts_mov_b32 %1, fd_code_begin-1b\n\ts_mov_b32 %2, fd_code_end-1b" : "=s"(pc), "=s"(beg), "=s"(end));
            const int off = (int)beg + ((b >> 3) * 64 + lane) * 8;
            if (off + 8 <= (int)end) code_pf = *(const GAS u32x2*)(pc + (long long)off);
        }
        const u64 e = get((GAS u64*)ws);
        const u32x4 xa = *(const GAS u32x4*)(x + 8 * lane), ga = *(const GAS u32x4*)(gw + 8 * lane);
        const int l2 = 64 + (lane & 15);
        const u32x4 xb = *(const GAS u32x4*)(x + 8 * l2), gb = *(const GAS u32x4*)(gw + 8 * l2);
        const u64 tag = (e + 1) & 0xFFFFFFFFFFull;
        const u32 thi = rfl((u32)(tag >> 8)), tlo = rfl((u32)tag & 255u);
        asm volatile("" :: "v"(code_pf));
        fl[F_THI] = thi; fl[F_TLO] = tlo;
        asm volatile("" ::: "memory");
        fl[F_TAG] = 1;
        float za = 0.f, zb = 0.f;
#pragma unroll
        for (int i = 0; i < 4; ++i) {
            za += rbf(bf2f(xa[i] & 0xffffu) * bf2f(ga[i] & 0xffffu)); za += rbf(__uint_as_float(xa[i] & 0xffff0000u) * __uint_as_float(ga[i] & 0xffff0000u));
            zb += rbf(bf2f(xb[i] & 0xffffu) * bf2f(gb[i] & 0xffffu)); zb += rbf(__uint_as_float(xb[i] & 0xffff0000u) * __uint_as_float(gb[i] & 0xffff0000u));
        }
        float z = za + (lane < 16 ? zb : 0.f);
        z += dpp<ROR8>(z); z += dpp<ROR4>(z); z += dpp<ROR2>(z); z += dpp<ROR1>(z);
        z = rbf(xrow_sum(z));
        const float gt = rbf(1.f / (1.f + __expf(-z)));
        fl[F_GATE] = __float_as_uint(gt); fl[F_GATE1] = __float_as_uint(rbf(1.f - gt));
        asm volatile("" ::: "memory");
        fl[F_TAG] = 2;
        if (h < 10) {
            const int r = 8 * h + s;
            wait_flag<2>(aprog, N_D + 10);
            u64 v[8];
            sweep<8>((GAS u64*)(ws + W_P) + (size_t)r * 512 + lane, 64, v, thi, tlo);
            float a = 0.f;
#pragma unroll
            for (int i = 0; i < 8; ++i) a += val(v[i]);
            a += dpp<ROR8>(a);
            a = xrow_sum(a);
            if (lane < 8) out[8 * r + lane] = (u16)f2bf(a);
            if (b == 0 && lane == 0) __hip_atomic_store((GAS u64*)ws, e + 1, __ATOMIC_RELAXED, __HIP_MEMORY_SCOPE_AGENT);
            STAMP(12);
        }
        return;
    }

    const int trow = 4 * g + q4;

    if (wave == 5) {
        if (lane < 10) *(LAS u32x4*)(lds + L_XG + 16 * lane) = *(const GAS u32x4*)(x + 80 * (gi >> 1) + 8 * lane);
        float r0, r1;
        xw_phase(lds, L_XG, L_G, 5, N_V, aprog, g, trow, p4, r0, r1);
        wait_flag<0>(afl + 4 * F_TAG, 2);
        const u32 thi = rfl(fl[F_THI]), tlo = rfl(fl[F_TLO]);
        GAS u64* gp = (GAS u64*)(ws + W_G) + (size_t)G * (16 * 96);
        put(gp + gi * 96 + lane, r0, thi, tlo);
        if (lane < 32) put(gp + gi * 96 + 64 + lane, r1, thi, tlo);
        STAMP(7);
        const int l = lane < 48 ? lane : lane - 48;
        const int mat = l >= 24, j = l - 24 * mat;
        u64 v[8];
        sweep<8>(gp + mat * 96 + 24 * (2 * (gi >> 3) + rh) + j, 192, v, thi, tlo SPARG(21));
        const float sm = rbf(((val(v[0]) + val(v[1])) + (val(v[2]) + val(v[3]))) + ((val(v[4]) + val(v[5])) + (val(v[6]) + val(v[7]))));
        const float gg = sm, uu = __shfl(sm, lane + 24);
        const float y = 0.7978845608028654f * (gg + 0.044715f * gg * gg * gg);
        const float th = 1.f - 2.f / (__expf(2.f * y) + 1.f);
        const float hid = rbf(0.5f * gg * (1.f + th)) * uu;
        if (lane < 32) *(LAS u16*)(lds + L_HS + 2 * lane) = lane < 24 ? (u16)f2bf(hid) : (u16)0;
        lds_inc((LAS u32*)(fl + F_H), lane);
        STAMP(8);
        return;
    }

    u32 thi, tlo;
    if (wave == 4) {
        if (lane < 20) *(LAS u32x4*)(lds + L_XS + 16 * lane) = *(const GAS u32x4*)(x + 160 * (s & 3) + 8 * lane);
        float r0, r1;
        xw_phase(lds, L_XS, L_XQ, 10, 0, aprog, g, trow, p4, r0, r1);
        wait_flag<0>(afl + 4 * F_TAG, 1);
        thi = rfl(fl[F_THI]); tlo = rfl(fl[F_TLO]);
        GAS u64* qp = (GAS u64*)(ws + W_Q) + (size_t)h * 768;
        put(qp + (s & 3) * 192 + 96 * (s >> 2) + lane, r0, thi, tlo);
        if (lane < 32) put(qp + (s & 3) * 192 + 96 * (s >> 2) + 64 + lane, r1, thi, tlo);
        lds_inc((LAS u32*)(fl + F_T1), lane);
        STAMP(2);
        u64 v[12];
        sweep<12>(qp + lane, 64, v, thi, tlo SPARG(26));
#pragma unroll
        for (int i = 0; i < 3; ++i) *(LAS u16*)(lds + L_QS + 2 * (64 * i + lane)) = (u16)f2bf((val(v[i]) + val(v[i + 3])) + (val(v[i + 6]) + val(v[i + 9])));
        lds_inc((LAS u32*)(fl + F_Q), lane);
        STAMP(3);
    } else {
        wait_flag<2>(afl + 4 * F_Q, 1);
        u32x4 qf[6];
#pragma unroll
        for (int st = 0; st < 6; ++st) qf[st] = *(LAS u32x4*)(lds + L_QS + 64 * st + 16 * g);
        u32 seen = 0;
        wait_ge<1>(aprog, N_A + 12 * (wave + 1), seen);
        float sc[8];
        const int xr = (c16 >> 1) & 7;
#pragma unroll
        for (int t = 0; t < 2; ++t) {
            LAS unsigned char* kp = lds + L_K + (32 * wave + 16 * t + c16) * 384;
            f32x4 acc = {0, 0, 0, 0};
#pragma unroll
            for (int st = 0; st < 6; ++st) {
                const int u = 4 * st + g;
                acc = mfma32(*(LAS u32x4*)(kp + 16 * (u ^ xr)), qf[st], acc);
            }
#pragma unroll
            for (int r = 0; r < 4; ++r) sc[4 * t + r] = rbf(rbf(acc[r]) * 0.07216878364870322f);
        }
        float mx = fmaxf(fmaxf(fmaxf(sc[0], sc[1]), fmaxf(sc[2], sc[3])), fmaxf(fmaxf(sc[4], sc[5]), fmaxf(sc[6], sc[7])));
        mx = xrow_max(mx);
        LAS float* mxs = (LAS float*)(lds + L_MX);
        if (lane == 0) mxs[wave] = mx;
        lds_inc((LAS u32*)(fl + F_MX), lane);
        wait_flag<0>(afl + 4 * F_MX, 4);
        const f32x4 m4 = *(volatile LAS f32x4*)mxs;
        const float m = fmaxf(fmaxf(m4[0], m4[1]), fmaxf(m4[2], m4[3]));
        float ls = 0.f;
        u32x4 pf;
#pragma unroll
        for (int i = 0; i < 4; ++i) {
            const float e0 = __expf(sc[2 * i] - m), e1 = __expf(sc[2 * i + 1] - m);
            ls += e0 + e1;
            pf[i] = f2bf(e0) | (f2bf(e1) << 16);
        }
        ls = xrow_sum(ls);
        if (c16 == 0) *(LAS u32x4*)(lds + L_PS + 64 * wave + 16 * g) = pf;
        if (lane == 0) mxs[4 + wave] = ls;
        lds_inc((LAS u32*)(fl + F_P), lane);
        thi = rfl(fl[F_THI]); tlo = rfl(fl[F_TLO]);
        if (wave < 3) {
            wait_flag<0>(afl + 4 * F_P, 4);
            if (wave == 0) STAMP(4);
            u32x4 af[4];
#pragma unroll
            for (int ks = 0; ks < 4; ++ks) af[ks] = *(LAS u32x4*)(lds + L_PS + 64 * ks + 16 * g);
            const f32x4 l4 = *(volatile LAS f32x4*)(mxs + 4);
            const float lsum = (l4[0] + l4[1]) + (l4[2] + l4[3]);
            f32x4 acc[4] = {{0, 0, 0, 0}, {0, 0, 0, 0}, {0, 0, 0, 0}, {0, 0, 0, 0}};
            const int vx = ((trow >> 1) & 3) << 1;
            LAS unsigned char* vp[4];
#pragma unroll
            for (int cc = 0; cc < 4; ++cc) vp[cc] = lds + L_V + trow * 384 + 16 * ((8 * wave + 2 * cc + (p4 >> 1)) ^ vx) + 8 * (p4 & 1);
#pragma unroll
            for (int ks = 0; ks < 4; ++ks) {
                wait_ge<1>(aprog, N_K + 12 * (ks + 1), seen);
#pragma unroll
                for (int cc = 0; cc < 4; ++cc) acc[cc] = mfma32(af[ks], trd2(vp[cc] + ks * 12288, 6144), acc[cc]);
            }
            const float r = g == 0 ? acc[0][0] : (g == 1 ? acc[1][0] : (g == 2 ? acc[2][0] : acc[3][0]));
            GAS u64* op = (GAS u64*)(ws + W_O) + (size_t)h * 8 * 192;
            GAS u64* mlp = (GAS u64*)(ws + W_ML) + h * 16;
            put(op + s * 192 + 64 * wave + lane, r, thi, tlo);
            if (wave == 0 && lane < 2) put(mlp + 2 * s + lane, lane == 0 ? m : lsum, thi, tlo);
            if (wave == 0) STAMP(5);
            if (wave < 2) {
                const int d = wave == 0 ? lane : 64 + (lane & 31);
                u64 v[9];
#pragma unroll 1
                for (int it = 0; it < SPIN_MEM; ++it) {
                    v[8] = get(mlp + c16);
#pragma unroll
                    for (int i = 0; i < 8; ++i) v[i] = get(op + i * 192 + 96 * rh + d);
                    u32 bd = 0;
#pragma unroll
                    for (int i = 0; i < 9; ++i) bd |= bad(v[i], thi, tlo);
                    if (__all(bd == 0)) break;
                }
                const float mv = val(v[8]);
                const float mi = dpp<0xA0>(mv), li = dpp<0xF5>(mv);
                float M = mi; M = fmaxf(M, dpp<ROR8>(M)); M = fmaxf(M, dpp<ROR4>(M)); M = fmaxf(M, dpp<ROR2>(M));
                const float ei = __expf(mi - M);
                float L = ei * li; L += dpp<ROR8>(L); L += dpp<ROR4>(L); L += dpp<ROR2>(L);
                const float wi = ei / L;
                float o = 0.f;
#pragma unroll
                for (int i = 0; i < 8; ++i) o = fmaf(__uint_as_float(__builtin_amdgcn_readlane(__float_as_uint(wi), 2 * i)), val(v[i]), o);
                *(LAS u16*)(lds + L_OS + 2 * d) = (u16)f2bf(o);
                lds_inc((LAS u32*)(fl + F_O), lane);
                if (wave == 0) STAMP(6);
            }
        }
    }

    {
        wait_flag<1>(afl + 4 * F_H, 1);
        const u32x2 ah0 = *(LAS u32x2*)(lds + L_HS + 8 * g), ah1 = *(LAS u32x2*)(lds + L_HS + 32 + 8 * g);
        u32 seen = 0;
        wait_ge<0>(aprog, N_D, seen);
        LAS unsigned char* dp = lds + L_D + 64 * wave + 8 * p4;
        const int r1 = 16 + (trow & 7);
        f32x4 b0 = {0, 0, 0, 0}, b1 = {0, 0, 0, 0}, a0 = {0, 0, 0, 0}, a1 = {0, 0, 0, 0};
        b0 = mfma16(ah0, trd(dp + trow * 320), b0);
        b1 = mfma16(ah0, trd(dp + trow * 320 + 32), b1);
        b0 = mfma16(ah1, trd(dp + r1 * 320), b0);
        b1 = mfma16(ah1, trd(dp + r1 * 320 + 32), b1);
        wait_flag<0>(afl + 4 * F_O, 2);
        u32x2 af[6];
#pragma unroll
        for (int u = 0; u < 6; ++u) af[u] = *(LAS u32x2*)(lds + L_OS + 32 * u + 8 * g);
        LAS unsigned char* bp = lds + L_XQ + trow * 320 + 64 * wave + 8 * p4;
#pragma unroll
        for (int u = 0; u < 6; ++u) {
            wait_ge<0>(aprog, N_D + 5 * (u + 1), seen);
            a0 = mfma16(af[u], trd(bp + u * 5120), a0);
            a1 = mfma16(af[u], trd(bp + u * 5120 + 32), a1);
        }
        const float gt = __uint_as_float(fl[F_GATE]), g1 = __uint_as_float(fl[F_GATE1]);
        const float r = g == 0 ? gt * a0[0] + g1 * b0[0] : gt * a1[0] + g1 * b1[0];
        if (lane < 32) put((GAS u64*)(ws + W_P) + ((size_t)(20 * cq + 4 * wave + (lane >> 3)) * 64 + 2 * h + rh) * 8 + (lane & 7), r, thi, tlo);
        if (wave == 0) STAMP(11);
    }
    asm volatile("fd_code_end:");
}

extern "C" void kernel_launch(void* const* d_in, const int* in_sizes, int n_in, void* d_out, int out_size, void* d_ws, size_t ws_size,
                              hipStream_t stream) {
    (void)hipFuncSetAttribute((const void*)fused_decode, hipFuncAttributeMaxDynamicSharedMemorySize, LDS_BYTES);
    fused_decode<<<256, 512, LDS_BYTES, stream>>>((const u16*)d_in[0], (const u16*)d_in[1], (const u16*)d_in[2], (const u16*)d_in[3], (const u16*)d_in[4],
                                                   (const u16*)d_in[6], (const u16*)d_in[7], (const u16*)d_in[8], (const u16*)d_in[9],
                                                   (u16*)d_out, (unsigned char*)d_ws
                                                   );
}
```

```cpp
#include <hip/hip_runtime.h>
#include <hip/hip_bf16.h>

typedef unsigned short u16;
typedef unsigned u32;
typedef unsigned long long u64;
typedef u32 u32x4 __attribute__((ext_vector_type(4)));
typedef u32 u32x2 __attribute__((ext_vector_type(2)));
typedef u64 u64x8 __attribute__((ext_vector_type(8)));
typedef float f32x4 __attribute__((ext_vector_type(4)));
typedef short s16x4 __attribute__((ext_vector_type(4)));
typedef __bf16 bf16x8 __attribute__((ext_vector_type(8)));
typedef __bf16 bf16x2 __attribute__((ext_vector_type(2)));
#define GAS __attribute__((address_space(1)))
#define LAS __attribute__((address_space(3)))

constexpr int SK = 4096, WN = 1024;

constexpr int PACE_A = 60;
constexpr int PACE_KV = 58;
constexpr int PACE_B = 60;
constexpr int BURST = 15;
constexpr int T_ANCHOR = 45;
constexpr int HOLD = 9;
constexpr int WINDOW = 40;
constexpr int DLY_Q = 105, DLY_O = 50, DLY_F = 30;

constexpr int L_XQ = 0;
constexpr int L_K  = 30720;
constexpr int L_V  = 79872;
constexpr int L_G  = 129024;
constexpr int L_D  = 144384;
constexpr int L_S  = 152576;
constexpr int L_XS = L_S;
constexpr int L_QS = L_S + 1152, L_PS = L_S + 1536, L_OS = L_S + 1792, L_HS = L_S + 2048, L_MX = L_S + 2112;
constexpr int L_PROG = L_S + 2304, L_FL = L_PROG + 256, LDS_BYTES = L_FL + 256;
enum { F_Q = 0, F_P, F_O, F_H, F_T1, F_TAG, F_THI, F_TLO, F_GATE, F_GATE1 };
constexpr size_t W_Q = 4096;
constexpr size_t W_G = W_Q + 32 * 4 * 192 * 8;
constexpr size_t W_O = W_G + 16 * 16 * 96 * 8;
constexpr size_t W_ML = W_O + 32 * 8 * 192 * 8;
constexpr size_t W_P = W_ML + 32 * 16 * 8;
constexpr int N_A = 30, N_K = N_A + 48, N_V = N_K + 48, N_G = N_V + 15, N_W = N_G + 30, N_ALL = N_W + 8;
constexpr int SPIN_LDS = 1 << 18, SPIN_MEM = 1 << 15;
constexpr float LOG2E = 1.4426950408889634f;

__device__ __forceinline__ float bf2f(u32 v) { return __uint_as_float(v << 16); }
__device__ __forceinline__ float bfhi(u32 v) { return __uint_as_float(v & 0xffff0000u); }
__device__ __forceinline__ u32 pk(float a, float b) { const bf16x2 v = {(__bf16)a, (__bf16)b}; return __builtin_bit_cast(u32, v); }
__device__ __forceinline__ float rbf(float a) { return bf2f(pk(a, a)); }
__device__ __forceinline__ u32 rfl(u32 v) { return __builtin_amdgcn_readfirstlane(v); }
__device__ __forceinline__ float ex2(float x) { return __builtin_amdgcn_exp2f(x); }
template <int CTRL> __device__ __forceinline__ float dpp(float x) {
    return __builtin_bit_cast(float, __builtin_amdgcn_mov_dpp(__builtin_bit_cast(int, x), CTRL, 0xf, 0xf, true));
}
constexpr int ROR8 = 0x128, ROR4 = 0x124, ROR2 = 0x122, ROR1 = 0x121;
__device__ __forceinline__ float xrow_max(float x) {
    auto s = __builtin_amdgcn_permlane16_swap(__float_as_uint(x), __float_as_uint(x), false, false);
    x = fmaxf(__uint_as_float(s[0]), __uint_as_float(s[1]));
    auto t = __builtin_amdgcn_permlane32_swap(__float_as_uint(x), __float_as_uint(x), false, false);
    return fmaxf(__uint_as_float(t[0]), __uint_as_float(t[1]));
}
__device__ __forceinline__ float xrow_sum(float x) {
    auto s = __builtin_amdgcn_permlane16_swap(__float_as_uint(x), __float_as_uint(x), false, false);
    x = __uint_as_float(s[0]) + __uint_as_float(s[1]);
    auto t = __builtin_amdgcn_permlane32_swap(__float_as_uint(x), __float_as_uint(x), false, false);
    return __uint_as_float(t[0]) + __uint_as_float(t[1]);
}
__device__ __forceinline__ float sel4(int g, float a, float b, float c, float d) {
    asm volatile("" : "+v"(a), "+v"(b), "+v"(c), "+v"(d));
    const float x = (g & 1) ? b : a, y = (g & 1) ? d : c;
    return (g & 2) ? y : x;
}
__device__ __forceinline__ float sel2(int g, float a, float b) { asm volatile("" : "+v"(a), "+v"(b)); return (g & 1) ? b : a; }
__device__ __forceinline__ f32x4 mfma32(u32x4 a, u32x4 b, f32x4 c) {
    return __builtin_amdgcn_mfma_f32_16x16x32_bf16(__builtin_bit_cast(bf16x8, a), __builtin_bit_cast(bf16x8, b), c, 0, 0, 0);
}
__device__ __forceinline__ f32x4 mfma16(u32x2 a, u32x2 b, f32x4 c) {
    return __builtin_amdgcn_mfma_f32_16x16x16bf16_1k(__builtin_bit_cast(s16x4, a), __builtin_bit_cast(s16x4, b), c, 0, 0, 0);
}
__device__ __forceinline__ u32x2 trd(LAS unsigned char* p) {
    return __builtin_bit_cast(u32x2, __builtin_amdgcn_ds_read_tr16_b64_v4i16((LAS s16x4*)p));
}
__device__ __forceinline__ u32x4 trd2(LAS unsigned char* p, int off2) {
    u32x2 a = trd(p), b = trd(p + off2);
    return u32x4{a.x, a.y, b.x, b.y};
}
template <int SLEEP> __device__ __forceinline__ void wait_ge(u32 addr, u32 need, u32& seen) {
    u32 tmp, cnt = SPIN_LDS;
    if (SLEEP)
        asm volatile("s_cmp_ge_u32 %0, %4\n\ts_cbranch_scc1 2f\n"
                     "1:\n\tds_read_b32 %1, %3\n\ts_waitcnt lgkmcnt(0)\n\tv_readfirstlane_b32 %0, %1\n\ts_cmp_ge_u32 %0, %4\n\ts_cbranch_scc1 2f\n"
                     "\ts_sleep %5\n\ts_sub_u32 %2, %2, 1\n\ts_cbranch_scc0 1b\n2:"
                     : "+s"(seen), "=&v"(tmp), "+s"(cnt) : "v"(addr), "s"(need), "i"(SLEEP) : "memory", "scc");
    else
        asm volatile("s_cmp_ge_u32 %0, %4\n\ts_cbranch_scc1 2f\n"
                     "1:\n\tds_read_b32 %1, %3\n\ts_waitcnt lgkmcnt(0)\n\tv_readfirstlane_b32 %0, %1\n\ts_cmp_ge_u32 %0, %4\n\ts_cbranch_scc1 2f\n"
                     "\ts_sub_u32 %2, %2, 1\n\ts_cbranch_scc0 1b\n2:"
                     : "+s"(seen), "=&v"(tmp), "+s"(cnt) : "v"(addr), "s"(need) : "memory", "scc");
}
template <int SLEEP> __device__ __forceinline__ void wait_flag(u32 addr, u32 need) { u32 seen = 0; wait_ge<SLEEP>(addr, need, seen); }
__device__ __forceinline__ void lds_inc(u32 addr) {
    u64 sv;
    asm volatile("s_mov_b64 %0, exec\n\ts_mov_b64 exec, 1\n\tds_add_u32 %1, %2\n\ts_mov_b64 exec, %0" : "=&s"(sv) : "v"(addr), "v"(1u) : "memory");
}
__device__ __forceinline__ void delay(int n) {
    const u32 t = (u32)__builtin_amdgcn_s_memrealtime();
#pragma unroll 1
    for (int it = 0; it < 4096; ++it) { if ((int)((u32)__builtin_amdgcn_s_memrealtime() - t) >= n) break; __builtin_amdgcn_s_sleep(1); }
}
__device__ __forceinline__ void put(GAS u64* g, float v, u32 thi, u32 tlo) {
    const u32 lo = __builtin_amdgcn_alignbit(tlo, __float_as_uint(v) + 0x80u, 8);
    __hip_atomic_store(g, ((u64)thi << 32) | lo, __ATOMIC_RELAXED, __HIP_MEMORY_SCOPE_AGENT);
}
__device__ __forceinline__ u64 get(GAS u64* g) { return __hip_atomic_load(g, __ATOMIC_RELAXED, __HIP_MEMORY_SCOPE_AGENT); }
__device__ __forceinline__ float val(u64 x) { return __uint_as_float((u32)x << 8); }
template <int N> __device__ __forceinline__ bool all_ok(const u64 (&v)[N], u32 thi, u32 tlo) {
    u32 bh = 0, bl = 0;
    const u32 t24 = tlo << 24;
#pragma unroll
    for (int i = 0; i < N; ++i) { bh |= (u32)(v[i] >> 32) ^ thi; bl |= (u32)v[i] ^ t24; }
    return __all((bh | (bl >> 24)) == 0);
}
template <int N> __device__ __forceinline__ void sweep(GAS u64* p, int S, u64 (&v)[N], u32 thi, u32 tlo) {
#pragma unroll 1
    for (int it = 0; it < SPIN_MEM; ++it) {
#pragma unroll
        for (int i = 0; i < N; ++i) v[i] = get(p + S * i);
        if (all_ok<N>(v, thi, tlo)) break;
    }
}

#define GL3(v0, v1, v2, base, ldsaddr) \
    asm volatile("s_memrealtime %0\n\ts_mov_b32 m0, %5\n\ts_nop 0\n\tglobal_load_lds_dwordx4 %1, %4 nt\n\tglobal_load_lds_dwordx4 %2, %4 offset:1024 nt\n\t" \
                 "global_load_lds_dwordx4 %3, %4 offset:2048 nt\n\ts_waitcnt lgkmcnt(0)" \
                 : "=&s"(tnow) : "v"(v0), "v"(v1), "v"(v2), "s"(base), "s"(ldsaddr) : "memory")
#define GL2(v0, v1, base, ldsaddr) \
    asm volatile("s_memrealtime %0\n\ts_mov_b32 m0, %4\n\ts_nop 0\n\tglobal_load_lds_dwordx4 %1, %3 nt\n\tglobal_load_lds_dwordx4 %2, %3 offset:1024 nt\n\t" \
                 "s_waitcnt lgkmcnt(0)" \
                 : "=&s"(tnow) : "v"(v0), "v"(v1), "s"(base), "s"(ldsaddr) : "memory")

struct XW { f32x4 a0, a1, a2; u32x2 a; LAS unsigned char* xp; LAS unsigned char* bp; u32 need, seen; };
__device__ __forceinline__ void xw_init(XW& w, LAS unsigned char* xp, LAS unsigned char* bp, u32 need) {
    w.a0 = f32x4{0, 0, 0, 0}; w.a1 = w.a0; w.a2 = w.a0; w.xp = xp; w.bp = bp; w.need = need; w.seen = 0;
    w.a = *(LAS u32x2*)xp;
}
template <int SLEEP> __device__ __forceinline__ void xw_run(XW& w, int nu, u32 aprog) {
#pragma unroll 1
    for (int u = 0; u < nu; ++u) {
        w.need += 3;
        wait_ge<SLEEP>(aprog, w.need, w.seen);
        w.xp += 32;
        const u32x2 an = *(LAS u32x2*)w.xp;
        const u32x2 b0 = trd(w.bp), b1 = trd(w.bp + 32), b2 = trd(w.bp + 64);
        __builtin_amdgcn_sched_barrier(0);
        w.a0 = mfma16(w.a, b0, w.a0); w.a1 = mfma16(w.a, b1, w.a1); w.a2 = mfma16(w.a, b2, w.a2);
        w.a = an; w.bp += 3072;
    }
}
__device__ __forceinline__ float xw_result(const XW& w, int g) { return sel4(g, w.a0[0], w.a1[0], w.a2[0], w.a2[0]); }

__global__ void __attribute__((amdgpu_flat_work_group_size(512, 512), amdgpu_waves_per_eu(2, 2)))
fused_decode(const u16* x_, const u16* kc_, const u16* vc_, const u16* wq_, const u16* wo_, const u16* wg_, const u16* wu_, const u16* wd_, const u16* __restrict__ gw,
             u16* __restrict__ out, unsigned char* __restrict__ ws) {
    extern __shared__ __attribute__((aligned(16))) unsigned char lds_[];
    LAS unsigned char* lds = (LAS unsigned char*)lds_;
    const int tid = threadIdx.x, lane = tid & 63;
    const int wave = rfl(tid >> 6);
    const int b = blockIdx.x;
    const int x8 = b & 7, jj = b >> 3, side = x8 >> 2, xx = x8 & 3, e = jj & 1, m = jj >> 1;
    const int h = 2 * m + side, s = 2 * xx + e;
    const int ho = 2 * m + 1 - side;
    const int rq = xx;
    const int rh = xx >> 1, cq = 2 * (xx & 1) + e;
    const int G = 2 * (m >> 1) + e, gi = 2 * x8 + (m & 1);
    const int beta = 2 * ho + rh;
    volatile LAS u32* prog = (volatile LAS u32*)(lds + L_PROG);
    volatile LAS u32* fl = (volatile LAS u32*)(lds + L_FL);
    const u32 aprog = (u32)(size_t)lds + L_PROG, afl = (u32)(size_t)lds + L_FL;
    if (wave == 6) { prog[lane] = 0; fl[lane] = 0; asm volatile("s_waitcnt lgkmcnt(0)" ::: "memory"); }
    const u64x8 ka = *(const volatile __attribute__((address_space(4))) u64x8*)__builtin_amdgcn_kernarg_segment_ptr();
    __builtin_amdgcn_s_barrier();
    const u16* x = (const u16*)ka[0]; const u16* kc = (const u16*)ka[1]; const u16* vc = (const u16*)ka[2]; const u16* wq = (const u16*)ka[3];
    const u16* wo = (const u16*)ka[4]; const u16* wg = (const u16*)ka[5]; const u16* wu = (const u16*)ka[6]; const u16* wd = (const u16*)ka[7];
    asm volatile("" ::: "memory");

    const int g = lane >> 4, c16 = lane & 15, q4 = c16 >> 2, p4 = lane & 3;

    if (wave == 7) {
        __builtin_amdgcn_s_setprio(3);
        const u32 t0 = (u32)__builtin_amdgcn_s_memrealtime();
        u32 issued = 0;
        const u32 lbase = (u32)(size_t)lds;
        auto publish = [&]() {
            const u32 r = __builtin_amdgcn_s_getreg(7 | (31 << 11));
            const u32 vm = (r & 15u) | ((r >> 18) & 0x30u);
            prog[lane] = issued - vm;
            return vm;
        };
        int target = (T_ANCHOR << 4) - BURST * PACE_A;
        u64 tnow = t0;
        u32 vml = 0;
        auto pace = [&](int n, int rate) {
            if (issued >= BURST) {
                int now = (int)(((u32)tnow - t0) << 4);
                if (issued == BURST + HOLD) {
#pragma unroll 1
                    for (int it = 0; it < SPIN_LDS; ++it) if (publish() < issued) break;
                    now = (int)(((u32)__builtin_amdgcn_s_memrealtime() - t0) << 4);
                    target = max(target, now);
                }
#pragma unroll 1
                while (now < target || vml > WINDOW) {
                    vml = publish();
                    now = (int)(((u32)__builtin_amdgcn_s_memrealtime() - t0) << 4);
                }
            }
            vml = publish() + n;
            target += n * rate;
        };
        u32 r12[3], c12[3];
#pragma unroll
        for (int i = 0; i < 3; ++i) { const u32 p = 64 * i + lane; r12[i] = p / 12; c12[i] = (p % 12) * 16 - 1024 * i; }
        {
            const char* base = (const char*)wq + (size_t)(160 * rq) * 12288 + 384 * ho + 192 * e;
            const u32 v0 = r12[0] * 12288 + c12[0], v1 = r12[1] * 12288 + c12[1], v2 = r12[2] * 12288 + c12[2];
            u32 la = lbase + L_XQ;
#pragma unroll 1
            for (int u = 0; u < 10; ++u) { pace(3, PACE_A); GL3(v0, v1, v2, base, la); issued += 3; base += 16 * 12288; la += 3072; }
        }
        {
            const char* base = (const char*)kc + ((size_t)h * SK + (SK - WN) + 128 * s) * 384;
            u32 kv[6];
#pragma unroll
            for (int i = 0; i < 6; ++i) { const u32 p = 64 * i + lane, key = p / 24, u = p % 24; kv[i] = key * 384 + ((u ^ ((key >> 1) & 7)) * 16) - 1024 * (i % 3); }
            u32 la = lbase + L_K;
#pragma unroll 1
            for (int u = 0; u < 8; ++u) {
                pace(3, PACE_KV); GL3(kv[0], kv[1], kv[2], base, la); issued += 3;
                pace(3, PACE_KV); GL3(kv[3], kv[4], kv[5], base, la + 3072); issued += 3;
                base += 6144; la += 6144;
            }
        }
        {
            const char* base = (const char*)vc + ((size_t)h * SK + (SK - WN) + 128 * s) * 384;
            u32 vv[3];
#pragma unroll
            for (int i = 0; i < 3; ++i) { const u32 p = 64 * i + lane, row = p / 24, u = p % 24; vv[i] = row * 384 + ((u ^ (((row >> 1) & 3) << 1)) * 16) - 1024 * i; }
            u32 la = lbase + L_V;
#pragma unroll 1
            for (int u = 0; u < 16; ++u) { pace(3, PACE_KV); GL3(vv[0], vv[1], vv[2], base, la); issued += 3; base += 3072; la += 3072; }
        }
        {
            const char* base = (const char*)((m & 1) ? wu : wg) + (size_t)(80 * x8) * 3072 + 192 * G;
            const u32 v0 = r12[0] * 3072 + c12[0], v1 = r12[1] * 3072 + c12[1], v2 = r12[2] * 3072 + c12[2];
            u32 la = lbase + L_G;
#pragma unroll 1
            for (int u = 0; u < 5; ++u) { pace(3, PACE_A); GL3(v0, v1, v2, base, la); issued += 3; base += 16 * 3072; la += 3072; }
        }
        {
            u32 ov[5];
#pragma unroll
            for (int i = 0; i < 5; ++i) { const u32 p = 64 * i + lane; ov[i] = (p / 20) * 1280 + (p % 20) * 16 - 1024 * (i % 3); }
            wait_flag<0>(afl + 4 * F_T1, 2);
            const char* base = (const char*)wo + (size_t)(192 * ho + 96 * rh) * 1280 + 320 * cq;
            u32 la = lbase + L_XQ;
#pragma unroll 1
            for (int u = 0; u < 6; ++u) {
                pace(3, PACE_B); GL3(ov[0], ov[1], ov[2], base, la); issued += 3;
                pace(2, PACE_B); GL2(ov[3], ov[4], base, la + 3072); issued += 2;
                base += 16 * 1280; la += 5120;
            }
            base = (const char*)wd + (size_t)(24 * beta) * 1280 + 320 * cq;
            la = lbase + L_D;
            pace(3, PACE_B); GL3(ov[0], ov[1], ov[2], base, la); issued += 3;
            pace(2, PACE_B); GL2(ov[3], ov[4], base, la + 3072); issued += 2;
            {
                const u32 p = 128 + (lane & 31);
                const u32 o2 = (p / 20) * 1280 + (p % 20) * 16 - 2048;
                const char* base2 = base + 16 * 1280;
                const u32 la2 = la + 5120;
                pace(3, PACE_B); GL3(ov[0], ov[1], o2, base2, la2); issued += 3;
            }
        }
#pragma unroll 1
        for (int it = 0; it < SPIN_LDS; ++it) if (publish() == 0) break;
        asm volatile("s_waitcnt vmcnt(0)" ::: "memory");
        prog[lane] = N_ALL;
        return;
    }

    const int trow = 4 * g + q4;
    u32 thi, tlo;
    u64 epoch = 0;

    if (wave >= 4) {
        const int hw = wave - 4;
        LAS unsigned char* xs = lds + L_XS + 576 * (hw & 1);
        if (wave == 6) {
            epoch = get((GAS u64*)ws);
            const u32x4 xa = *(const GAS u32x4*)(x + 8 * lane), ga = *(const GAS u32x4*)(gw + 8 * lane);
            const int l2 = 64 + (lane & 15);
            const u32x4 xb = *(const GAS u32x4*)(x + 8 * l2), gb = *(const GAS u32x4*)(gw + 8 * l2);
            const u64 tag = (epoch + 1) & 0xFFFFFFFFFFull;
            thi = rfl((u32)(tag >> 8)); tlo = rfl((u32)tag & 255u);
            fl[F_THI] = thi; fl[F_TLO] = tlo;
            asm volatile("" ::: "memory");
            fl[F_TAG] = 1;
            __builtin_amdgcn_sched_barrier(0);
            float za = 0.f, zb = 0.f;
#pragma unroll
            for (int i = 0; i < 4; ++i) {
                const u32 pa = pk(bf2f(xa[i]) * bf2f(ga[i]), bfhi(xa[i]) * bfhi(ga[i])), pb = pk(bf2f(xb[i]) * bf2f(gb[i]), bfhi(xb[i]) * bfhi(gb[i]));
                za += bf2f(pa) + bfhi(pa); zb += bf2f(pb) + bfhi(pb);
            }
            float z = za + (lane < 16 ? zb : 0.f);
            z += dpp<ROR8>(z); z += dpp<ROR4>(z); z += dpp<ROR2>(z); z += dpp<ROR1>(z);
            z = rbf(xrow_sum(z));
            const float gt = rbf(1.f / (1.f + __expf(-z)));
            fl[F_GATE] = __float_as_uint(gt); fl[F_GATE1] = __float_as_uint(rbf(1.f - gt));
            asm volatile("" ::: "memory");
            fl[F_TAG] = 2;
            wait_flag<2>(afl + 4 * F_T1, 2);
        } else {
            if (lane < 20) *(LAS u32x4*)(xs + 16 * lane) = *(const GAS u32x4*)(x + 160 * rq + 8 * lane);
            else if (lane < 30) *(LAS u32x4*)(xs + 352 + 16 * (lane - 20)) = *(const GAS u32x4*)(x + 80 * x8 + 8 * (lane - 20));
            XW w;
            xw_init(w, xs + 8 * g, lds + L_XQ + trow * 192 + 96 * hw + 8 * p4, 0);
            xw_run<1>(w, 8, aprog);
            wait_flag<0>(afl + 4 * F_TAG, 1);
            thi = rfl(fl[F_THI]); tlo = rfl(fl[F_TLO]);
            GAS u64* qp = (GAS u64*)(ws + W_Q) + (size_t)ho * 768 + rq * 192 + 96 * e + 48 * hw + lane;
            asm volatile("" : "+v"(qp));
            xw_run<0>(w, 2, aprog);
            if (lane < 48) put(qp, xw_result(w, g), thi, tlo);
            lds_inc(afl + 4 * F_T1);
        }
        {
            delay(DLY_Q);
            u64 v[4];
            sweep<4>((GAS u64*)(ws + W_Q) + (size_t)h * 768 + 64 * hw + lane, 192, v, thi, tlo);
            *(LAS u16*)(lds + L_QS + 2 * (64 * hw + lane)) = (u16)pk((val(v[0]) + val(v[1])) + (val(v[2]) + val(v[3])), 0.f);
            lds_inc(afl + 4 * F_Q);
        }
        if (wave < 6) {
            GAS u64* gp = (GAS u64*)(ws + W_G) + (size_t)G * (16 * 96) + gi * 96 + 48 * hw + lane;
            const int l5 = lane & 31;
            GAS u64* gq = (GAS u64*)(ws + W_G) + (size_t)(beta >> 2) * (16 * 96) + (lane >> 5) * 96 + 24 * (beta & 3) + (l5 < 24 ? l5 : 0);
            asm volatile("" : "+v"(gp), "+v"(gq));
            XW w;
            xw_init(w, xs + 352 + 8 * g, lds + L_G + trow * 192 + 96 * hw + 8 * p4, N_V);
            xw_run<1>(w, 3, aprog);
            xw_run<0>(w, 2, aprog);
            if (lane < 48) put(gp, xw_result(w, g), thi, tlo);
            if (wave == 5) {
                delay(DLY_F);
                u64 v[8];
                sweep<8>(gq, 192, v, thi, tlo);
                const float gg = rbf(((val(v[0]) + val(v[1])) + (val(v[2]) + val(v[3]))) + ((val(v[4]) + val(v[5])) + (val(v[6]) + val(v[7]))));
                const auto sw = __builtin_amdgcn_permlane32_swap(__float_as_uint(gg), __float_as_uint(gg), false, false);
                const float uu = __uint_as_float(sw[1]);
                const float y2 = (2.f * LOG2E * 0.7978845608028654f) * (gg + 0.044715f * gg * gg * gg);
                const float th = 1.f - 2.f * __builtin_amdgcn_rcpf(ex2(y2) + 1.f);
                const float hid = rbf(0.5f * gg * (1.f + th)) * uu;
                if (lane < 32) *(LAS u16*)(lds + L_HS + 2 * lane) = lane < 24 ? (u16)pk(hid, hid) : (u16)0;
                lds_inc(afl + 4 * F_H);
                return;
            }
        }
    } else {
        u32 seen = 0;
        wait_ge<2>(aprog, N_A + 12 * (wave + 1), seen);
        u32x4 kf[12];
        {
            const int xr = (c16 >> 1) & 7;
            LAS unsigned char* kp = lds + L_K + (32 * wave + c16) * 384;
#pragma unroll
            for (int st = 0; st < 6; ++st) {
                const int o = 16 * ((4 * st + g) ^ xr);
                kf[st] = *(LAS u32x4*)(kp + o); kf[6 + st] = *(LAS u32x4*)(kp + 6144 + o);
            }
        }
        wait_flag<1>(afl + 4 * F_Q, 3);
        u32x4 qf[6];
#pragma unroll
        for (int st = 0; st < 6; ++st) qf[st] = *(LAS u32x4*)(lds + L_QS + 64 * st + 16 * g);
        __builtin_amdgcn_sched_barrier(0);
        f32x4 s0 = {0, 0, 0, 0}, s1 = {0, 0, 0, 0};
#pragma unroll
        for (int st = 0; st < 6; ++st) { s0 = mfma32(kf[st], qf[st], s0); s1 = mfma32(kf[6 + st], qf[st], s1); }
        float sc[8];
#pragma unroll
        for (int i = 0; i < 4; ++i) {
            const f32x4& sv = i < 2 ? s0 : s1;
            const u32 a = pk(sv[2 * (i & 1)], sv[2 * (i & 1) + 1]);
            const u32 c = pk(bf2f(a) * 0.07216878364870322f, bfhi(a) * 0.07216878364870322f);
            sc[2 * i] = bf2f(c); sc[2 * i + 1] = bfhi(c);
        }
        const float mw = xrow_max(fmaxf(fmaxf(fmaxf(sc[0], sc[1]), fmaxf(sc[2], sc[3])), fmaxf(fmaxf(sc[4], sc[5]), fmaxf(sc[6], sc[7]))));
        const float mneg = -mw * LOG2E;
        float ls = 0.f;
        u32x4 pf;
#pragma unroll
        for (int i = 0; i < 4; ++i) {
            const float e0 = ex2(fmaf(sc[2 * i], LOG2E, mneg)), e1 = ex2(fmaf(sc[2 * i + 1], LOG2E, mneg));
            ls += e0 + e1;
            pf[i] = pk(e0, e1);
        }
        ls = xrow_sum(ls);
        if (c16 == 0) *(LAS u32x4*)(lds + L_PS + 64 * wave + 16 * g) = pf;
        if (lane < 2) *(LAS float*)(lds + L_MX + 16 * lane + 4 * wave) = lane == 0 ? mw : ls;
        lds_inc(afl + 4 * F_P);
        thi = rfl(fl[F_THI]); tlo = rfl(fl[F_TLO]);
        if (wave < 3) {
            const int vx = ((trow >> 1) & 3) << 1;
            LAS unsigned char* vp[4];
#pragma unroll
            for (int cc = 0; cc < 4; ++cc) vp[cc] = lds + L_V + trow * 384 + 16 * ((8 * wave + 2 * cc + (p4 >> 1)) ^ vx) + 8 * (p4 & 1);
            u32x4 bv[12];
#pragma unroll
            for (int ks = 0; ks < 3; ++ks) {
                wait_ge<1>(aprog, N_K + 12 * (ks + 1), seen);
#pragma unroll
                for (int cc = 0; cc < 4; ++cc) bv[4 * ks + cc] = trd2(vp[cc] + ks * 12288, 6144);
            }
            wait_flag<0>(afl + 4 * F_P, 4);
            u32x4 af[4];
#pragma unroll
            for (int ks = 0; ks < 4; ++ks) af[ks] = *(LAS u32x4*)(lds + L_PS + 64 * ks + 16 * g);
            const f32x4 m4 = *(LAS f32x4*)(lds + L_MX), l4 = *(LAS f32x4*)(lds + L_MX + 16);
            __builtin_amdgcn_sched_barrier(0);
            f32x4 acc[12];
#pragma unroll
            for (int i = 0; i < 12; ++i) acc[i] = mfma32(af[i >> 2], bv[i], f32x4{0, 0, 0, 0});
            const float mh = fmaxf(fmaxf(m4[0], m4[1]), fmaxf(m4[2], m4[3]));
            float cw[4];
#pragma unroll
            for (int ks = 0; ks < 4; ++ks) cw[ks] = ex2((m4[ks] - mh) * LOG2E);
            const float lsum = (cw[0] * l4[0] + cw[1] * l4[1]) + (cw[2] * l4[2] + cw[3] * l4[3]);
            float r = 0.f;
#pragma unroll
            for (int ks = 0; ks < 3; ++ks)
                r = fmaf(cw[ks], sel4(g, acc[4 * ks][0], acc[4 * ks + 1][0], acc[4 * ks + 2][0], acc[4 * ks + 3][0]), r);
            if (wave == 0 && lane < 2) put((GAS u64*)(ws + W_ML) + h * 16 + 2 * s + lane, lane == 0 ? mh : lsum, thi, tlo);
            GAS u64* op = (GAS u64*)(ws + W_O) + (size_t)h * 8 * 192 + s * 192 + 64 * wave + lane;
            asm volatile("" : "+v"(r), "+v"(op));
            wait_ge<0>(aprog, N_V, seen);
            {
                u32x4 bl[4];
#pragma unroll
                for (int cc = 0; cc < 4; ++cc) bl[cc] = trd2(vp[cc] + 3 * 12288, 6144);
                __builtin_amdgcn_sched_barrier(0);
                f32x4 al[4];
#pragma unroll
                for (int cc = 0; cc < 4; ++cc) al[cc] = mfma32(af[3], bl[cc], f32x4{0, 0, 0, 0});
                r = fmaf(cw[3], sel4(g, al[0][0], al[1][0], al[2][0], al[3][0]), r);
            }
            put(op, r, thi, tlo);
            if (wave < 2) {
                const int d = wave == 0 ? lane : 64 + (lane & 31);
                GAS u64* oq = (GAS u64*)(ws + W_O) + (size_t)ho * 8 * 192 + 96 * rh + d;
                GAS u64* mlq = (GAS u64*)(ws + W_ML) + ho * 16 + c16;
                asm volatile("" : "+v"(oq), "+v"(mlq));
                auto weights = [&](u64 ml) {
                    const float mv = val(ml);
                    const float mi = dpp<0xA0>(mv), li = dpp<0xF5>(mv);
                    float M = mi; M = fmaxf(M, dpp<ROR8>(M)); M = fmaxf(M, dpp<ROR4>(M)); M = fmaxf(M, dpp<ROR2>(M));
                    const float ei = ex2((mi - M) * LOG2E);
                    float L = ei * li; L += dpp<ROR8>(L); L += dpp<ROR4>(L); L += dpp<ROR2>(L);
                    return ei * __builtin_amdgcn_rcpf(L);
                };
                u64 v[9];
                v[8] = get(mlq);
                delay(DLY_O);
#pragma unroll
                for (int i = 0; i < 8; ++i) v[i] = get(oq + i * 192);
                __builtin_amdgcn_sched_barrier(0);
                float wi = weights(v[8]);
                asm volatile("" : "+v"(wi));
                if (!all_ok<9>(v, thi, tlo)) {
#pragma unroll 1
                    for (int it = 0; it < SPIN_MEM; ++it) {
#pragma unroll
                        for (int i = 0; i < 8; ++i) v[i] = get(oq + i * 192);
                        v[8] = get(mlq);
                        if (all_ok<9>(v, thi, tlo)) break;
                    }
                    wi = weights(v[8]);
                }
                float o0 = 0.f, o1 = 0.f;
#pragma unroll
                for (int i = 0; i < 4; ++i) {
                    o0 = fmaf(__uint_as_float(__builtin_amdgcn_readlane(__float_as_uint(wi), 4 * i)), val(v[2 * i]), o0);
                    o1 = fmaf(__uint_as_float(__builtin_amdgcn_readlane(__float_as_uint(wi), 4 * i + 2)), val(v[2 * i + 1]), o1);
                }
                *(LAS u16*)(lds + L_OS + 2 * d) = (u16)pk(o0 + o1, 0.f);
                lds_inc(afl + 4 * F_O);
                if (wave == 0) return;
            }
        }
    }

    {
        const int j = wave < 5 ? wave - 1 : 4;
        u32 seen = 0;
        GAS u64* pp = (GAS u64*)(ws + W_P) + ((size_t)(20 * cq + 4 * j + ((lane & 31) >> 3)) * 64 + 2 * ho + rh) * 8 + (lane & 7);
        asm volatile("" : "+v"(pp));
        LAS unsigned char* bp = lds + L_XQ + trow * 320 + 64 * j + 8 * p4;
        u32x2 bw[12];
#pragma unroll
        for (int u = 0; u < 6; ++u) {
            wait_ge<1>(aprog, N_G + 5 * (u + 1), seen);
            bw[2 * u] = trd(bp + u * 5120); bw[2 * u + 1] = trd(bp + u * 5120 + 32);
        }
        wait_flag<0>(afl + 4 * F_O, 2);
        u32x2 af[6];
#pragma unroll
        for (int u = 0; u < 6; ++u) af[u] = *(LAS u32x2*)(lds + L_OS + 32 * u + 8 * g);
        wait_flag<0>(afl + 4 * F_TAG, 2);
        const float gt = __uint_as_float(fl[F_GATE]), g1 = __uint_as_float(fl[F_GATE1]);
        __builtin_amdgcn_sched_barrier(0);
        f32x4 a0 = {0, 0, 0, 0}, a1 = {0, 0, 0, 0}, b0 = {0, 0, 0, 0}, b1 = {0, 0, 0, 0};
#pragma unroll
        for (int k = 0; k < 3; ++k) {
            const u32x4 a = {af[2 * k].x, af[2 * k].y, af[2 * k + 1].x, af[2 * k + 1].y};
            a0 = mfma32(a, u32x4{bw[4 * k].x, bw[4 * k].y, bw[4 * k + 2].x, bw[4 * k + 2].y}, a0);
            a1 = mfma32(a, u32x4{bw[4 * k + 1].x, bw[4 * k + 1].y, bw[4 * k + 3].x, bw[4 * k + 3].y}, a1);
        }
        float ra = gt * sel2(g, a0[0], a1[0]);
        asm volatile("" : "+v"(ra));
        wait_ge<0>(aprog, N_ALL, seen);
        LAS unsigned char* dp = lds + L_D + 64 * j + 8 * p4;
        const int r1 = 16 + (trow & 7);
        const u32x2 d00 = trd(dp + trow * 320), d01 = trd(dp + trow * 320 + 32), d10 = trd(dp + r1 * 320), d11 = trd(dp + r1 * 320 + 32);
        wait_flag<0>(afl + 4 * F_H, 1);
        const u32x2 ah0 = *(LAS u32x2*)(lds + L_HS + 8 * g), ah1 = *(LAS u32x2*)(lds + L_HS + 32 + 8 * g);
        __builtin_amdgcn_sched_barrier(0);
        const u32x4 ah = {ah0.x, ah0.y, ah1.x, ah1.y};
        b0 = mfma32(ah, u32x4{d00.x, d00.y, d10.x, d10.y}, b0);
        b1 = mfma32(ah, u32x4{d01.x, d01.y, d11.x, d11.y}, b1);
        if (lane < 32) put(pp, fmaf(g1, sel2(g, b0[0], b1[0]), ra), thi, tlo);
    }
    if (wave == 6 && b < 80) {
        u64 v[8];
        sweep<8>((GAS u64*)(ws + W_P) + (size_t)b * 512 + lane, 64, v, thi, tlo);
        float a = ((val(v[0]) + val(v[1])) + (val(v[2]) + val(v[3]))) + ((val(v[4]) + val(v[5])) + (val(v[6]) + val(v[7])));
        a += dpp<ROR8>(a);
        a = xrow_sum(a);
        if (lane < 8) __hip_atomic_store((GAS u16*)out + 8 * b + lane, (u16)pk(a, a), __ATOMIC_RELAXED, __HIP_MEMORY_SCOPE_AGENT);
        if (b == 0 && lane == 0) __hip_atomic_store((GAS u64*)ws, epoch + 1, __ATOMIC_RELAXED, __HIP_MEMORY_SCOPE_AGENT);
    }
}

extern "C" void kernel_launch(void* const* d_in, const int* in_sizes, int n_in, void* d_out, int out_size, void* d_ws, size_t ws_size,
                              hipStream_t stream) {
    (void)in_sizes; (void)n_in; (void)out_size; (void)ws_size;
    (void)hipFuncSetAttribute((const void*)fused_decode, hipFuncAttributeMaxDynamicSharedMemorySize, LDS_BYTES);
    fused_decode<<<256, 512, LDS_BYTES, stream>>>((const u16*)d_in[0], (const u16*)d_in[1], (const u16*)d_in[2], (const u16*)d_in[3], (const u16*)d_in[4],
                                                   (const u16*)d_in[6], (const u16*)d_in[7], (const u16*)d_in[8], (const u16*)d_in[9],
                                                   (u16*)d_out, (unsigned char*)d_ws);
}
```
